```python
import torch

def _softmax(x, axis=-1):
    return torch.softmax(x, dim=axis)
def _gelu(x):
    return torch.nn.functional.gelu(x, approximate="tanh")
def _silu(x):
    return torch.nn.functional.silu(x)
def _sigmoid(x):
    return torch.sigmoid(x)
def _layernorm(x, axis=-1, eps=1e-5):
    m = x.mean(dim=axis, keepdim=True)
    v = x.var(dim=axis, keepdim=True, unbiased=False)
    return (x - m) / torch.sqrt(v + eps)
def _scatter_add(tgt, idx, src, axis=0):
    return tgt.clone().index_add_(axis, idx, src)
def _segment_sum(src, seg, n):
    out = torch.zeros((n,) + src.shape[1:], dtype=src.dtype)
    return out.index_add_(0, seg, src)
def _pad(x, pw):
    flat = tuple(v for pair in reversed(pw) for v in pair)
    return torch.nn.functional.pad(x, flat)

kernel_name = 'standalone__mha_block__prompt'

def setup_inputs():

    _w = (lambda a: (lambda t: t.to(torch.bfloat16) if t.is_floating_point() else t)(torch.as_tensor(a)))
    B, S, D, H = 1, 2048, 640, 32
    return {
        "x": _w((lambda *s: torch.randn(*s, dtype=torch.bfloat16) * 0.2)(B, S, D)),
        "wqkv": _w((lambda *s: torch.randn(*s, dtype=torch.bfloat16) * 0.2)(D, 3 * D)),
        "wo": _w((lambda *s: torch.randn(*s, dtype=torch.bfloat16) * 0.05)(D, D)),
    }

def reference(x, wqkv, wo):
    B, S, D = x.shape
    H = 32
    Dh = D // H
    qkv = (lambda x, *d: x.permute(*d))((x @ wqkv).reshape(B, S, 3, H, Dh), 2, 0, 3, 1, 4)
    q, k, v = qkv[0], qkv[1], qkv[2]
    scores = torch.einsum("bhqd,bhkd->bhqk", q, k) * (Dh ** -0.5)
    p = _softmax(scores, axis=-1)
    o = torch.einsum("bhqk,bhkd->bhqd", p, v)
    return (lambda x, *d: x.permute(*d))(o, 0, 2, 1, 3).reshape(B, S, D) @ wo
```

```cpp
#include <hip/hip_runtime.h>
#include <hip/hip_bf16.h>
#include <stdint.h>

typedef unsigned short u16;
typedef __attribute__((ext_vector_type(8))) __bf16 bf16x8;
typedef __attribute__((ext_vector_type(4))) __bf16 bf16x4;
typedef __attribute__((ext_vector_type(2))) __bf16 bf16x2;
typedef __attribute__((ext_vector_type(4))) short s16x4;
typedef __attribute__((ext_vector_type(16))) float f32x16;
typedef __attribute__((ext_vector_type(2))) float f32x2;
typedef __attribute__((ext_vector_type(4))) unsigned u32x4;
typedef __attribute__((ext_vector_type(2))) unsigned u32x2;

#define S_LEN 2048
#define D_MODEL 640
#define NHEAD 32
#define DHEAD 20
#define N_QKV 1920

#define MFMA32(a, b, c) __builtin_amdgcn_mfma_f32_32x32x16_bf16((a), (b), (c), 0, 0, 0)

__device__ __forceinline__ u16 f2bf(float f) {
  __bf16 b = (__bf16)f;
  return __builtin_bit_cast(u16, b);
}

template <int BM, int BN, class Epi>
__global__ __launch_bounds__(256) void gemm_kernel(const u16* __restrict__ A, const u16* __restrict__ B,
                                                   int N, int K, int nTilesN, Epi epi) {
  constexpr int BK = 64;
  constexpr int AS = BK + 8;
  constexpr int BS = BN + 32;
  constexpr int TM = BM / 64;
  constexpr int TN = BN / 64;
  constexpr int ACH = BM * BK / 8 / 256;
  constexpr int BCH = BK * BN / 8 / 256;
  __shared__ __attribute__((aligned(16))) u16 As[2][BM * AS];
  __shared__ __attribute__((aligned(16))) u16 Bs[2][BK * BS];

  const int tid = threadIdx.x;
  const int lane = tid & 63;
  const int wave = tid >> 6;
  const int wm = wave >> 1, wn = wave & 1;
  const int r = lane & 31, hh = lane >> 5;
  const int bm = blockIdx.x / nTilesN, bn = blockIdx.x % nTilesN;
  const int m0 = bm * BM, n0 = bn * BN;

  u32x4 ra[ACH], rb[BCH];
  auto gload = [&](int k0) {
#pragma unroll
    for (int i = 0; i < ACH; ++i) {
      int c = tid + 256 * i;
      int row = c >> 3, c8 = c & 7;
      ra[i] = *(const u32x4*)&A[(size_t)(m0 + row) * K + k0 + c8 * 8];
    }
#pragma unroll
    for (int i = 0; i < BCH; ++i) {
      int c = tid + 256 * i;
      int row = c / (BN / 8), c8 = c % (BN / 8);
      rb[i] = *(const u32x4*)&B[(size_t)(k0 + row) * N + n0 + c8 * 8];
    }
  };
  auto lstore = [&](int buf) {
#pragma unroll
    for (int i = 0; i < ACH; ++i) {
      int c = tid + 256 * i;
      int row = c >> 3, c8 = c & 7;
      *(u32x4*)&As[buf][row * AS + c8 * 8] = ra[i];
    }
#pragma unroll
    for (int i = 0; i < BCH; ++i) {
      int c = tid + 256 * i;
      int row = c / (BN / 8), c8 = c % (BN / 8);
      *(u32x4*)&Bs[buf][row * BS + c8 * 8] = rb[i];
    }
  };

  f32x16 acc[TM][TN];
#pragma unroll
  for (int i = 0; i < TM; ++i)
#pragma unroll
    for (int j = 0; j < TN; ++j)
#pragma unroll
      for (int e = 0; e < 16; ++e) acc[i][j][e] = 0.f;

  const int g = lane >> 4, gi = lane & 15;
  const int tq = gi >> 2, tp = gi & 3;

  const int nk = K / BK;
  gload(0);
  lstore(0);
  __syncthreads();
  for (int kt = 0; kt < nk; ++kt) {
    const int buf = kt & 1;
    if (kt + 1 < nk) gload((kt + 1) * BK);
#pragma unroll
    for (int ks = 0; ks < BK / 16; ++ks) {
      bf16x8 a[TM], b[TN];
#pragma unroll
      for (int i = 0; i < TM; ++i)
        a[i] = *(const bf16x8*)&As[buf][(wm * (BM / 2) + i * 32 + r) * AS + ks * 16 + 8 * hh];
#pragma unroll
      for (int j = 0; j < TN; ++j) {
        const int col = wn * (BN / 2) + j * 32 + (g & 1) * 16 + 4 * tp;
        const int krow = ks * 16 + 8 * (g >> 1) + tq;
        s16x4 lo = __builtin_amdgcn_ds_read_tr16_b64_v4i16(
            (__attribute__((address_space(3))) s16x4*)&Bs[buf][krow * BS + col]);
        s16x4 hi = __builtin_amdgcn_ds_read_tr16_b64_v4i16(
            (__attribute__((address_space(3))) s16x4*)&Bs[buf][(krow + 4) * BS + col]);
        typedef __attribute__((ext_vector_type(8))) short s16x8;
        s16x8 t = {lo[0], lo[1], lo[2], lo[3], hi[0], hi[1], hi[2], hi[3]};
        b[j] = __builtin_bit_cast(bf16x8, t);
      }
#pragma unroll
      for (int i = 0; i < TM; ++i)
#pragma unroll
        for (int j = 0; j < TN; ++j) acc[i][j] = MFMA32(a[i], b[j], acc[i][j]);
    }
    if (kt + 1 < nk) lstore(buf ^ 1);
    __syncthreads();
  }

#pragma unroll
  for (int i = 0; i < TM; ++i)
#pragma unroll
    for (int j = 0; j < TN; ++j)
#pragma unroll
      for (int e = 0; e < 16; ++e) {
        const int m = m0 + wm * (BM / 2) + i * 32 + (e & 3) + 8 * (e >> 2) + 4 * hh;
        const int n = n0 + wn * (BN / 2) + j * 32 + r;
        epi(m, n, acc[i][j][e]);
      }
}

struct EpiQKV {
  u16* qs;
  u16* kf;
  u16* vf;
  __device__ __forceinline__ void operator()(int m, int n, float v) const {
    const int which = n / D_MODEL;
    const int c = n - which * D_MODEL;
    const int h = c / DHEAD, d = c - h * DHEAD;
    const int key = m & 31;
    const size_t tbase = (size_t)(h * 64 + (m >> 5)) * 640;
    if (which == 0) {
      qs[(size_t)m * D_MODEL + c] = f2bf(v * (0.22360679774997896f * 1.4426950408889634f));
    } else if (which == 1) {
      const int off = (d < 16) ? ((key + 32 * (d >> 3)) * 8 + (d & 7)) : (512 + key * 4 + (d - 16));
      kf[tbase + off] = f2bf(v);
    } else {
      const int s = key >> 4, w = key & 15;
      const int hh = (w >> 2) & 1, j = (w & 3) + 4 * (w >> 3);
      vf[tbase + ((s * 2 + hh) * 20 + d) * 8 + j] = f2bf(v);
    }
  }
};

struct EpiOut {
  u16* out;
  __device__ __forceinline__ void operator()(int m, int n, float v) const {
    out[(size_t)m * D_MODEL + n] = f2bf(v);
  }
};

#define CH_KEYS 256
#define CH_TILES 8
#define NCHUNK (S_LEN / CH_KEYS)

__device__ __forceinline__ float max16(const f32x16& v) {
  float a = fmaxf(fmaxf(v[0], v[1]), v[2]);
  a = fmaxf(fmaxf(a, v[3]), v[4]);
  a = fmaxf(fmaxf(a, v[5]), v[6]);
  a = fmaxf(fmaxf(a, v[7]), v[8]);
  a = fmaxf(fmaxf(a, v[9]), v[10]);
  a = fmaxf(fmaxf(a, v[11]), v[12]);
  a = fmaxf(fmaxf(a, v[13]), v[14]);
  return fmaxf(a, v[15]);
}

__global__ __launch_bounds__(256) void attn_kernel(const u16* __restrict__ qs, const u16* __restrict__ kf,
                                                   const u16* __restrict__ vf, u16* __restrict__ o) {
  __shared__ __attribute__((aligned(16))) u16 Ks[2][CH_TILES * 640];
  __shared__ __attribute__((aligned(16))) u16 Vs[2][CH_TILES * 4 * 32 * 8];

  const int tid = threadIdx.x, lane = tid & 63, wave = tid >> 6;
  const int r = lane & 31, hh = lane >> 5;
  const int xcd = blockIdx.x & 7, idx = blockIdx.x >> 3;
  const int h = xcd * 4 + (idx >> 4), qb = idx & 15;
  const int q = qb * 128 + wave * 32 + r;

  for (int i = tid; i < 2 * CH_TILES * 4 * 12; i += 256) {
    const int buf = i / (CH_TILES * 4 * 12), rem = i % (CH_TILES * 4 * 12);
    const int ts = rem / 12, d = 20 + rem % 12;
    const unsigned val = (d == 20) ? 0x3F803F80u : 0u;
    u32x4 v4 = {val, val, val, val};
    *(u32x4*)&Vs[buf][(ts * 32 + d) * 8] = v4;
  }

  bf16x8 qf0, qf1;
  {
    const u16* qp = qs + (size_t)q * D_MODEL + h * DHEAD;
    u32x2 a0 = *(const u32x2*)(qp + 8 * hh);
    u32x2 a1 = *(const u32x2*)(qp + 8 * hh + 4);
    u32x4 t0 = {a0[0], a0[1], a1[0], a1[1]};
    qf0 = __builtin_bit_cast(bf16x8, t0);
    u32x2 b0 = *(const u32x2*)(qp + 16);
    u32x4 t1 = {hh ? 0u : b0[0], hh ? 0u : b0[1], 0u, 0u};
    qf1 = __builtin_bit_cast(bf16x8, t1);
  }

  const u16* kg = kf + (size_t)h * 64 * 640;
  const u16* vg = vf + (size_t)h * 64 * 640;
  u32x4 rk[3], rv[3];
  auto gload = [&](int c) {
#pragma unroll
    for (int i = 0; i < 3; ++i) {
      const int id = tid + 256 * i;
      if (id < 640) {
        rk[i] = *(const u32x4*)&kg[(size_t)c * 5120 + id * 8];
        rv[i] = *(const u32x4*)&vg[(size_t)c * 5120 + id * 8];
      }
    }
  };
  auto lstore = [&](int buf) {
#pragma unroll
    for (int i = 0; i < 3; ++i) {
      const int id = tid + 256 * i;
      if (id < 640) {
        *(u32x4*)&Ks[buf][id * 8] = rk[i];
        const int ts = id / 20, d = id % 20;
        *(u32x4*)&Vs[buf][(ts * 32 + d) * 8] = rv[i];
      }
    }
  };

  f32x16 oacc, cvec;
#pragma unroll
  for (int e = 0; e < 16; ++e) { oacc[e] = 0.f; cvec[e] = 0.f; }
  float mrun = 0.f;
  const float THR = 6.0f;

  gload(0);
  lstore(0);
  __syncthreads();
  for (int c = 0; c < NCHUNK; ++c) {
    const int buf = c & 1;
    if (c + 1 < NCHUNK) gload(c + 1);
    for (int t = 0; t < CH_TILES; ++t) {
      const bf16x8 kf0 = *(const bf16x8*)&Ks[buf][t * 640 + lane * 8];
      const u32x2 k1 = *(const u32x2*)&Ks[buf][t * 640 + 512 + r * 4];
      const u32x4 k1v = {k1[0], k1[1], 0u, 0u};
      const bf16x8 kf1 = __builtin_bit_cast(bf16x8, k1v);
      f32x16 sacc = MFMA32(kf0, qf0, cvec);
      sacc = MFMA32(kf1, qf1, sacc);
      float tmax = max16(sacc);
      const bool first = (c == 0 && t == 0);
      if (first || __any(tmax > THR)) {
        float dm = fmaxf(tmax, __shfl_xor(tmax, 32));
        if (!first) dm = fmaxf(dm, 0.f);
        const float alpha = exp2f(-dm);
#pragma unroll
        for (int e = 0; e < 16; ++e) {
          oacc[e] = first ? 0.f : oacc[e] * alpha;
          sacc[e] -= dm;
        }
        mrun += dm;
#pragma unroll
        for (int e = 0; e < 16; ++e) cvec[e] = -mrun;
      }
      typedef __attribute__((ext_vector_type(4))) unsigned u4;
      u4 p0, p1;
#pragma unroll
      for (int e = 0; e < 4; ++e) {
        f32x2 a = {exp2f(sacc[2 * e]), exp2f(sacc[2 * e + 1])};
        p0[e] = __builtin_bit_cast(unsigned, __builtin_convertvector(a, bf16x2));
        f32x2 b = {exp2f(sacc[8 + 2 * e]), exp2f(sacc[8 + 2 * e + 1])};
        p1[e] = __builtin_bit_cast(unsigned, __builtin_convertvector(b, bf16x2));
      }
      const bf16x8 vf0 = *(const bf16x8*)&Vs[buf][((t * 4 + 0 + hh) * 32 + r) * 8];
      const bf16x8 vf1 = *(const bf16x8*)&Vs[buf][((t * 4 + 2 + hh) * 32 + r) * 8];
      oacc = MFMA32(vf0, __builtin_bit_cast(bf16x8, p0), oacc);
      oacc = MFMA32(vf1, __builtin_bit_cast(bf16x8, p1), oacc);
    }
    if (c + 1 < NCHUNK) lstore(buf ^ 1);
    __syncthreads();
  }

  float l = __shfl(oacc[8], r + 32);
  const float inv = 1.0f / l;
  u16* op = o + (size_t)q * D_MODEL + h * DHEAD;
#pragma unroll
  for (int gq = 0; gq < 3; ++gq) {
    if (gq < 2 || hh == 0) {
      f32x2 a = {oacc[4 * gq] * inv, oacc[4 * gq + 1] * inv};
      f32x2 b = {oacc[4 * gq + 2] * inv, oacc[4 * gq + 3] * inv};
      u32x2 w = {__builtin_bit_cast(unsigned, __builtin_convertvector(a, bf16x2)),
                 __builtin_bit_cast(unsigned, __builtin_convertvector(b, bf16x2))};
      *(u32x2*)(op + 8 * gq + 4 * hh) = w;
    }
  }
}

extern "C" void kernel_launch(void* const* d_in, const int* in_sizes, int n_in, void* d_out, int out_size,
                              void* d_ws, size_t ws_size, hipStream_t stream) {
  const u16* x = (const u16*)d_in[0];
  const u16* wqkv = (const u16*)d_in[1];
  const u16* wo = (const u16*)d_in[2];
  u16* out = (u16*)d_out;
  u16* ws = (u16*)d_ws;
  u16* qs = ws;
  u16* kf = qs + (size_t)S_LEN * D_MODEL;
  u16* vf = kf + (size_t)S_LEN * D_MODEL;
  u16* ob = vf + (size_t)S_LEN * D_MODEL;

  EpiQKV e1{qs, kf, vf};
  gemm_kernel<128, 128, EpiQKV><<<dim3((S_LEN / 128) * (N_QKV / 128)), dim3(256), 0, stream>>>(
      x, wqkv, N_QKV, D_MODEL, N_QKV / 128, e1);
  attn_kernel<<<dim3(NHEAD * (S_LEN / 128)), dim3(256), 0, stream>>>(qs, kf, vf, ob);
  EpiOut e3{out};
  gemm_kernel<128, 64, EpiOut><<<dim3((S_LEN / 128) * (D_MODEL / 64)), dim3(256), 0, stream>>>(
      ob, wo, D_MODEL, D_MODEL, D_MODEL / 64, e3);
}
```

```cpp
#include <hip/hip_runtime.h>
#include <hip/hip_bf16.h>
#include <stdint.h>

typedef unsigned short u16;
typedef __attribute__((ext_vector_type(8))) __bf16 bf16x8;
typedef __attribute__((ext_vector_type(4))) __bf16 bf16x4;
typedef __attribute__((ext_vector_type(2))) __bf16 bf16x2;
typedef __attribute__((ext_vector_type(4))) short s16x4;
typedef __attribute__((ext_vector_type(16))) float f32x16;
typedef __attribute__((ext_vector_type(2))) float f32x2;
typedef __attribute__((ext_vector_type(4))) unsigned u32x4;
typedef __attribute__((ext_vector_type(2))) unsigned u32x2;

#define S_LEN 2048
#define D_MODEL 640
#define NHEAD 32
#define DHEAD 20
#define N_QKV 1920

#define MFMA32(a, b, c) __builtin_amdgcn_mfma_f32_32x32x16_bf16((a), (b), (c), 0, 0, 0)

__device__ __forceinline__ u16 f2bf(float f) {
  __bf16 b = (__bf16)f;
  return __builtin_bit_cast(u16, b);
}

template <int BM, int BN, bool SWAP, class Epi>
__device__ __forceinline__ void gemm_body(const u16* __restrict__ A, const u16* __restrict__ B, int N, int K,
                                          int m0, int n0, const Epi& epi, u16* As, u16* Bs) {
  constexpr int BK = 64;
  constexpr int AS = BK + 8;
  constexpr int BS = BN + 32;
  constexpr int TM = BM / 64;
  constexpr int TN = BN / 64;
  constexpr int ACH = BM * BK / 8 / 256;
  constexpr int BCH = BK * BN / 8 / 256;

  const int tid = threadIdx.x;
  const int lane = tid & 63;
  const int wave = tid >> 6;
  const int wm = wave >> 1, wn = wave & 1;
  const int r = lane & 31, hh = lane >> 5;

  u32x4 ra[2][ACH], rb[2][BCH];
  auto gload = [&](int k0, int sl) {
#pragma unroll
    for (int i = 0; i < ACH; ++i) {
      int c = tid + 256 * i;
      int row = c >> 3, c8 = c & 7;
      ra[sl][i] = *(const u32x4*)&A[(size_t)(m0 + row) * K + k0 + c8 * 8];
    }
#pragma unroll
    for (int i = 0; i < BCH; ++i) {
      int c = tid + 256 * i;
      int row = c / (BN / 8), c8 = c % (BN / 8);
      rb[sl][i] = *(const u32x4*)&B[(size_t)(k0 + row) * N + n0 + c8 * 8];
    }
  };
  auto lstore = [&](int buf, int sl) {
#pragma unroll
    for (int i = 0; i < ACH; ++i) {
      int c = tid + 256 * i;
      int row = c >> 3, c8 = c & 7;
      *(u32x4*)&As[buf * (BM * AS) + row * AS + c8 * 8] = ra[sl][i];
    }
#pragma unroll
    for (int i = 0; i < BCH; ++i) {
      int c = tid + 256 * i;
      int row = c / (BN / 8), c8 = c % (BN / 8);
      *(u32x4*)&Bs[buf * (BK * BS) + row * BS + c8 * 8] = rb[sl][i];
    }
  };

  f32x16 acc[TM][TN];
#pragma unroll
  for (int i = 0; i < TM; ++i)
#pragma unroll
    for (int j = 0; j < TN; ++j)
#pragma unroll
      for (int e = 0; e < 16; ++e) acc[i][j][e] = 0.f;

  const int g = lane >> 4, gi = lane & 15;
  const int tq = gi >> 2, tp = gi & 3;

  const int nk = K / BK;
  gload(0, 0);
  gload(BK, 1);
  lstore(0, 0);
  __syncthreads();
  for (int kt = 0; kt < nk; ++kt) {
    const int buf = kt & 1;
    if (kt + 2 < nk) gload((kt + 2) * BK, buf);
#pragma unroll
    for (int ks = 0; ks < BK / 16; ++ks) {
      bf16x8 a[TM], b[TN];
#pragma unroll
      for (int i = 0; i < TM; ++i)
        a[i] = *(const bf16x8*)&As[buf * (BM * AS) + (wm * (BM / 2) + i * 32 + r) * AS + ks * 16 + 8 * hh];
#pragma unroll
      for (int j = 0; j < TN; ++j) {
        const int col = wn * (BN / 2) + j * 32 + (g & 1) * 16 + 4 * tp;
        const int krow = ks * 16 + 8 * (g >> 1) + tq;
        s16x4 lo = __builtin_amdgcn_ds_read_tr16_b64_v4i16(
            (__attribute__((address_space(3))) s16x4*)&Bs[buf * (BK * BS) + krow * BS + col]);
        s16x4 hi = __builtin_amdgcn_ds_read_tr16_b64_v4i16(
            (__attribute__((address_space(3))) s16x4*)&Bs[buf * (BK * BS) + (krow + 4) * BS + col]);
        typedef __attribute__((ext_vector_type(8))) short s16x8;
        s16x8 t = {lo[0], lo[1], lo[2], lo[3], hi[0], hi[1], hi[2], hi[3]};
        b[j] = __builtin_bit_cast(bf16x8, t);
      }
#pragma unroll
      for (int i = 0; i < TM; ++i)
#pragma unroll
        for (int j = 0; j < TN; ++j)
          acc[i][j] = SWAP ? MFMA32(b[j], a[i], acc[i][j]) : MFMA32(a[i], b[j], acc[i][j]);
    }
    if (kt + 1 < nk) lstore(buf ^ 1, buf ^ 1);
    __syncthreads();
  }

#pragma unroll
  for (int i = 0; i < TM; ++i)
#pragma unroll
    for (int j = 0; j < TN; ++j)
#pragma unroll
      for (int g4 = 0; g4 < 4; ++g4) {
        f32x2 lo = {acc[i][j][4 * g4], acc[i][j][4 * g4 + 1]};
        f32x2 hi = {acc[i][j][4 * g4 + 2], acc[i][j][4 * g4 + 3]};
        if (SWAP) {
          const int m = m0 + wm * (BM / 2) + i * 32 + r;
          const int n = n0 + wn * (BN / 2) + j * 32 + 8 * g4 + 4 * hh;
          epi.vecN(m, n, lo, hi);
        } else {
          const int m = m0 + wm * (BM / 2) + i * 32 + 8 * g4 + 4 * hh;
          const int n = n0 + wn * (BN / 2) + j * 32 + r;
          epi.vecM(m, n, lo, hi);
        }
      }
}

__device__ __forceinline__ u32x2 pack4(f32x2 lo, f32x2 hi) {
  u32x2 w = {__builtin_bit_cast(unsigned, __builtin_convertvector(lo, bf16x2)),
             __builtin_bit_cast(unsigned, __builtin_convertvector(hi, bf16x2))};
  return w;
}

struct EpiQKV {
  u16* qs;
  u16* kf;
  u16* vf;
  __device__ __forceinline__ void vecN(int m, int n, f32x2 lo, f32x2 hi) const {
    const int which = n / D_MODEL;
    const int c = n - which * D_MODEL;
    const int h = c / DHEAD, d = c - h * DHEAD;
    const int key = m & 31;
    if (which == 0) {
      const float sc = 0.22360679774997896f * 1.4426950408889634f;
      *(u32x2*)&qs[(size_t)m * D_MODEL + c] = pack4(lo * sc, hi * sc);
    } else {
      const size_t tbase = (size_t)(h * 64 + (m >> 5)) * 640;
      const int off = (d < 16) ? ((key + 32 * (d >> 3)) * 8 + (d & 7)) : (512 + key * 4);
      *(u32x2*)&kf[tbase + off] = pack4(lo, hi);
    }
  }
  __device__ __forceinline__ void vecM(int m, int n, f32x2 lo, f32x2 hi) const {
    const int c = n - 2 * D_MODEL;
    const int h = c / DHEAD, d = c - h * DHEAD;
    const int key = m & 31;
    const int s = key >> 4, w = key & 15;
    const int hh = (w >> 2) & 1, j0 = 4 * (w >> 3);
    const size_t tbase = (size_t)(h * 64 + (m >> 5)) * 640;
    *(u32x2*)&vf[tbase + ((s * 2 + hh) * 20 + d) * 8 + j0] = pack4(lo, hi);
  }
};

struct EpiOut {
  u16* out;
  __device__ __forceinline__ void vecN(int m, int n, f32x2 lo, f32x2 hi) const {
    *(u32x2*)&out[(size_t)m * D_MODEL + n] = pack4(lo, hi);
  }
  __device__ __forceinline__ void vecM(int m, int n, f32x2 lo, f32x2 hi) const {}
};

__global__ __launch_bounds__(256, 2) void qkv_kernel(const u16* __restrict__ A, const u16* __restrict__ B, EpiQKV epi) {
  constexpr int BM = 128, BN = 128;
  __shared__ __attribute__((aligned(16))) u16 As[2 * BM * 72];
  __shared__ __attribute__((aligned(16))) u16 Bs[2 * 64 * (BN + 32)];
  const int nTN = N_QKV / BN;
  const int m0 = (blockIdx.x / nTN) * BM, n0 = (blockIdx.x % nTN) * BN;
  if (n0 < 2 * D_MODEL) gemm_body<BM, BN, true>(A, B, N_QKV, D_MODEL, m0, n0, epi, As, Bs);
  else gemm_body<BM, BN, false>(A, B, N_QKV, D_MODEL, m0, n0, epi, As, Bs);
}

__global__ __launch_bounds__(256, 2) void oproj_kernel(const u16* __restrict__ A, const u16* __restrict__ B, EpiOut epi) {
  constexpr int BM = 64, BN = 64;
  __shared__ __attribute__((aligned(16))) u16 As[2 * BM * 72];
  __shared__ __attribute__((aligned(16))) u16 Bs[2 * 64 * (BN + 32)];
  const int nTN = D_MODEL / BN;
  const int m0 = (blockIdx.x / nTN) * BM, n0 = (blockIdx.x % nTN) * BN;
  gemm_body<BM, BN, true>(A, B, D_MODEL, D_MODEL, m0, n0, epi, As, Bs);
}

#define CH_KEYS 256
#define CH_TILES 8
#define NCHUNK (S_LEN / CH_KEYS)

__device__ __forceinline__ float max3f(float a, float b, float c) { return fmaxf(fmaxf(a, b), c); }
__device__ __forceinline__ float max16(const f32x16& v) {
  const float a = max3f(v[0], v[1], v[2]), b = max3f(v[3], v[4], v[5]), c = max3f(v[6], v[7], v[8]);
  const float d = max3f(v[9], v[10], v[11]), e = max3f(v[12], v[13], v[14]);
  return fmaxf(max3f(a, b, c), max3f(d, e, v[15]));
}

__device__ __forceinline__ bf16x8 ld_kf0(const u16* Kt, int lane) { return *(const bf16x8*)&Kt[lane * 8]; }
__device__ __forceinline__ bf16x8 ld_kf1(const u16* Kt, int r) {
  const u32x2 k1 = *(const u32x2*)&Kt[512 + r * 4];
  const u32x4 k1v = {k1[0], k1[1], 0u, 0u};
  return __builtin_bit_cast(bf16x8, k1v);
}

__global__ __launch_bounds__(256, 2) void attn_kernel(const u16* __restrict__ qs, const u16* __restrict__ kf,
                                                      const u16* __restrict__ vf, u16* __restrict__ o) {
  __shared__ __attribute__((aligned(16))) u16 Ks[2][CH_TILES * 640];
  __shared__ __attribute__((aligned(16))) u16 Vs[2][CH_TILES * 4 * 32 * 8];

  const int tid = threadIdx.x, lane = tid & 63, wave = tid >> 6;
  const int r = lane & 31, hh = lane >> 5;
  const int xcd = blockIdx.x & 7, idx = blockIdx.x >> 3;
  const int h = xcd * 4 + (idx >> 4), qb = idx & 15;
  const int q = qb * 128 + wave * 32 + r;

  const u16* kg = kf + (size_t)h * 64 * 640;
  const u16* vg = vf + (size_t)h * 64 * 640;
  u32x4 rk[3], rv[3];
  auto gload = [&](int c) {
#pragma unroll
    for (int i = 0; i < 3; ++i) {
      const int id = tid + 256 * i;
      if (id < 640) {
        rk[i] = *(const u32x4*)&kg[(size_t)c * 5120 + id * 8];
        rv[i] = *(const u32x4*)&vg[(size_t)c * 5120 + id * 8];
      }
    }
  };
  auto lstore = [&](int buf) {
#pragma unroll
    for (int i = 0; i < 3; ++i) {
      const int id = tid + 256 * i;
      if (id < 640) {
        *(u32x4*)&Ks[buf][id * 8] = rk[i];
        const int ts = id / 20, d = id % 20;
        *(u32x4*)&Vs[buf][(ts * 32 + d) * 8] = rv[i];
      }
    }
  };
  gload(0);

  for (int i = tid; i < 2 * CH_TILES * 4 * 12; i += 256) {
    const int buf = i / (CH_TILES * 4 * 12), rem = i % (CH_TILES * 4 * 12);
    const int ts = rem / 12, d = 20 + rem % 12;
    const unsigned val = (d == 20) ? 0x3F803F80u : 0u;
    u32x4 v4 = {val, val, val, val};
    *(u32x4*)&Vs[buf][(ts * 32 + d) * 8] = v4;
  }

  bf16x8 qf0, qf1;
  {
    const u16* qp = qs + (size_t)q * D_MODEL + h * DHEAD;
    u32x2 a0 = *(const u32x2*)(qp + 8 * hh);
    u32x2 a1 = *(const u32x2*)(qp + 8 * hh + 4);
    u32x4 t0 = {a0[0], a0[1], a1[0], a1[1]};
    qf0 = __builtin_bit_cast(bf16x8, t0);
    u32x2 b0 = *(const u32x2*)(qp + 16);
    u32x4 t1 = {hh ? 0u : b0[0], hh ? 0u : b0[1], 0u, 0u};
    qf1 = __builtin_bit_cast(bf16x8, t1);
  }

  f32x16 oacc, cvec, scur, snext;
#pragma unroll
  for (int e = 0; e < 16; ++e) { oacc[e] = 0.f; cvec[e] = 0.f; }
  const float THR = 6.0f;

  lstore(0);
  __syncthreads();

  float mrun;
  {
    scur = MFMA32(ld_kf0(&Ks[0][0], lane), qf0, cvec);
    scur = MFMA32(ld_kf1(&Ks[0][0], r), qf1, scur);
    float tmax = max16(scur);
    mrun = fmaxf(tmax, __shfl_xor(tmax, 32));
#pragma unroll
    for (int e = 0; e < 16; ++e) { scur[e] -= mrun; cvec[e] = -mrun; }
  }

  for (int c = 0; c < NCHUNK; ++c) {
    const int buf = c & 1;
    if (c + 1 < NCHUNK) gload(c + 1);
    bf16x8 kA = ld_kf0(&Ks[buf][640], lane), kB = ld_kf1(&Ks[buf][640], r);
    bf16x8 vA = *(const bf16x8*)&Vs[buf][((0 + hh) * 32 + r) * 8];
    bf16x8 vB = *(const bf16x8*)&Vs[buf][((2 + hh) * 32 + r) * 8];
#pragma unroll
    for (int t = 0; t < CH_TILES; ++t) {
      if (t + 1 < CH_TILES) {
        snext = MFMA32(kA, qf0, cvec);
        snext = MFMA32(kB, qf1, snext);
      }
      const bf16x8 vf0 = vA, vf1 = vB;
      if (t + 2 < CH_TILES) {
        kA = ld_kf0(&Ks[buf][(t + 2) * 640], lane);
        kB = ld_kf1(&Ks[buf][(t + 2) * 640], r);
      }
      if (t + 1 < CH_TILES) {
        vA = *(const bf16x8*)&Vs[buf][(((t + 1) * 4 + 0 + hh) * 32 + r) * 8];
        vB = *(const bf16x8*)&Vs[buf][(((t + 1) * 4 + 2 + hh) * 32 + r) * 8];
      }
      float tmax = max16(scur);
      if (__any(tmax > THR)) {
        float dm = fmaxf(fmaxf(tmax, __shfl_xor(tmax, 32)), 0.f);
        const float alpha = __builtin_amdgcn_exp2f(-dm);
        mrun += dm;
#pragma unroll
        for (int e = 0; e < 16; ++e) {
          oacc[e] *= alpha;
          scur[e] -= dm;
          if (t + 1 < CH_TILES) snext[e] -= dm;
          cvec[e] = -mrun;
        }
      }
      typedef __attribute__((ext_vector_type(4))) unsigned u4;
      u4 p0, p1;
#pragma unroll
      for (int e = 0; e < 4; ++e) {
        f32x2 a = {__builtin_amdgcn_exp2f(scur[2 * e]), __builtin_amdgcn_exp2f(scur[2 * e + 1])};
        p0[e] = __builtin_bit_cast(unsigned, __builtin_convertvector(a, bf16x2));
        f32x2 b = {__builtin_amdgcn_exp2f(scur[8 + 2 * e]), __builtin_amdgcn_exp2f(scur[8 + 2 * e + 1])};
        p1[e] = __builtin_bit_cast(unsigned, __builtin_convertvector(b, bf16x2));
      }
      oacc = MFMA32(vf0, __builtin_bit_cast(bf16x8, p0), oacc);
      oacc = MFMA32(vf1, __builtin_bit_cast(bf16x8, p1), oacc);
      if (t + 1 < CH_TILES) scur = snext;
    }
    if (c + 1 < NCHUNK) {
      lstore(buf ^ 1);
      __syncthreads();
      scur = MFMA32(ld_kf0(&Ks[buf ^ 1][0], lane), qf0, cvec);
      scur = MFMA32(ld_kf1(&Ks[buf ^ 1][0], r), qf1, scur);
    }
  }

  float l = __shfl(oacc[8], r + 32);
  const float inv = 1.0f / l;
  u16* op = o + (size_t)q * D_MODEL + h * DHEAD;
#pragma unroll
  for (int gq = 0; gq < 3; ++gq) {
    if (gq < 2 || hh == 0) {
      f32x2 a = {oacc[4 * gq] * inv, oacc[4 * gq + 1] * inv};
      f32x2 b = {oacc[4 * gq + 2] * inv, oacc[4 * gq + 3] * inv};
      u32x2 w = {__builtin_bit_cast(unsigned, __builtin_convertvector(a, bf16x2)),
                 __builtin_bit_cast(unsigned, __builtin_convertvector(b, bf16x2))};
      *(u32x2*)(op + 8 * gq + 4 * hh) = w;
    }
  }
}

extern "C" void kernel_launch(void* const* d_in, const int* in_sizes, int n_in, void* d_out, int out_size,
                              void* d_ws, size_t ws_size, hipStream_t stream) {
  const u16* x = (const u16*)d_in[0];
  const u16* wqkv = (const u16*)d_in[1];
  const u16* wo = (const u16*)d_in[2];
  u16* out = (u16*)d_out;
  u16* ws = (u16*)d_ws;
  u16* qs = ws;
  u16* kf = qs + (size_t)S_LEN * D_MODEL;
  u16* vf = kf + (size_t)S_LEN * D_MODEL;
  u16* ob = vf + (size_t)S_LEN * D_MODEL;

  EpiQKV e1{qs, kf, vf};
  qkv_kernel<<<dim3((S_LEN / 128) * (N_QKV / 128)), dim3(256), 0, stream>>>(x, wqkv, e1);
  attn_kernel<<<dim3(NHEAD * (S_LEN / 128)), dim3(256), 0, stream>>>(qs, kf, vf, ob);
  EpiOut e3{out};
  oproj_kernel<<<dim3((S_LEN / 64) * (D_MODEL / 64)), dim3(256), 0, stream>>>(ob, wo, e3);
}
```

```cpp
#include <hip/hip_runtime.h>
#include <hip/hip_bf16.h>
#include <stdint.h>
#include <type_traits>

typedef unsigned short u16;
typedef __attribute__((ext_vector_type(8))) __bf16 bf16x8;
typedef __attribute__((ext_vector_type(4))) __bf16 bf16x4;
typedef __attribute__((ext_vector_type(2))) __bf16 bf16x2;
typedef __attribute__((ext_vector_type(4))) short s16x4;
typedef __attribute__((ext_vector_type(16))) float f32x16;
typedef __attribute__((ext_vector_type(2))) float f32x2;
typedef __attribute__((ext_vector_type(4))) unsigned u32x4;
typedef __attribute__((ext_vector_type(2))) unsigned u32x2;

#define S_LEN 2048
#define D_MODEL 640
#define NHEAD 32
#define DHEAD 20
#define N_QKV 1920

#define MFMA32(a, b, c) __builtin_amdgcn_mfma_f32_32x32x16_bf16((a), (b), (c), 0, 0, 0)

__device__ __forceinline__ u16 f2bf(float f) {
  __bf16 b = (__bf16)f;
  return __builtin_bit_cast(u16, b);
}

template <int BM, int BN, bool SWAP, class Epi>
__device__ __forceinline__ void gemm_body(const u16* __restrict__ A, const u16* __restrict__ B, int N, int K,
                                          int m0, int n0, const Epi& epi, u16* As, u16* Bs) {
  constexpr int BK = 64;
  constexpr int AS = BK + 8;
  constexpr int BS = BN + 32;
  constexpr int TM = BM / 64;
  constexpr int TN = BN / 64;
  constexpr int ACH = BM * BK / 8 / 256;
  constexpr int BCH = BK * BN / 8 / 256;

  const int tid = threadIdx.x;
  const int lane = tid & 63;
  const int wave = tid >> 6;
  const int wm = wave >> 1, wn = wave & 1;
  const int r = lane & 31, hh = lane >> 5;

  u32x4 ra[2][ACH], rb[2][BCH];
  auto gload = [&](int k0, int sl) {
#pragma unroll
    for (int i = 0; i < ACH; ++i) {
      int c = tid + 256 * i;
      int row = c >> 3, c8 = c & 7;
      ra[sl][i] = *(const u32x4*)&A[(size_t)(m0 + row) * K + k0 + c8 * 8];
    }
#pragma unroll
    for (int i = 0; i < BCH; ++i) {
      int c = tid + 256 * i;
      int row = c / (BN / 8), c8 = c % (BN / 8);
      rb[sl][i] = *(const u32x4*)&B[(size_t)(k0 + row) * N + n0 + c8 * 8];
    }
  };
  auto lstore = [&](int buf, int sl) {
#pragma unroll
    for (int i = 0; i < ACH; ++i) {
      int c = tid + 256 * i;
      int row = c >> 3, c8 = c & 7;
      *(u32x4*)&As[buf * (BM * AS) + row * AS + c8 * 8] = ra[sl][i];
    }
#pragma unroll
    for (int i = 0; i < BCH; ++i) {
      int c = tid + 256 * i;
      int row = c / (BN / 8), c8 = c % (BN / 8);
      *(u32x4*)&Bs[buf * (BK * BS) + row * BS + c8 * 8] = rb[sl][i];
    }
  };

  f32x16 acc[TM][TN];
#pragma unroll
  for (int i = 0; i < TM; ++i)
#pragma unroll
    for (int j = 0; j < TN; ++j)
#pragma unroll
      for (int e = 0; e < 16; ++e) acc[i][j][e] = 0.f;

  const int g = lane >> 4, gi = lane & 15;
  const int tq = gi >> 2, tp = gi & 3;

  const int nk = K / BK;
  gload(0, 0);
  gload(BK, 1);
  lstore(0, 0);
  __syncthreads();
  for (int kt = 0; kt < nk; ++kt) {
    const int buf = kt & 1;
    if (kt + 2 < nk) gload((kt + 2) * BK, buf);
#pragma unroll
    for (int ks = 0; ks < BK / 16; ++ks) {
      bf16x8 a[TM], b[TN];
#pragma unroll
      for (int i = 0; i < TM; ++i)
        a[i] = *(const bf16x8*)&As[buf * (BM * AS) + (wm * (BM / 2) + i * 32 + r) * AS + ks * 16 + 8 * hh];
#pragma unroll
      for (int j = 0; j < TN; ++j) {
        const int col = wn * (BN / 2) + j * 32 + (g & 1) * 16 + 4 * tp;
        const int krow = ks * 16 + 8 * (g >> 1) + tq;
        s16x4 lo = __builtin_amdgcn_ds_read_tr16_b64_v4i16(
            (__attribute__((address_space(3))) s16x4*)&Bs[buf * (BK * BS) + krow * BS + col]);
        s16x4 hi = __builtin_amdgcn_ds_read_tr16_b64_v4i16(
            (__attribute__((address_space(3))) s16x4*)&Bs[buf * (BK * BS) + (krow + 4) * BS + col]);
        typedef __attribute__((ext_vector_type(8))) short s16x8;
        s16x8 t = {lo[0], lo[1], lo[2], lo[3], hi[0], hi[1], hi[2], hi[3]};
        b[j] = __builtin_bit_cast(bf16x8, t);
      }
#pragma unroll
      for (int i = 0; i < TM; ++i)
#pragma unroll
        for (int j = 0; j < TN; ++j)
          acc[i][j] = SWAP ? MFMA32(b[j], a[i], acc[i][j]) : MFMA32(a[i], b[j], acc[i][j]);
    }
    if (kt + 1 < nk) lstore(buf ^ 1, buf ^ 1);
    __syncthreads();
  }

#pragma unroll
  for (int i = 0; i < TM; ++i)
#pragma unroll
    for (int j = 0; j < TN; ++j)
#pragma unroll
      for (int g4 = 0; g4 < 4; ++g4) {
        f32x2 lo = {acc[i][j][4 * g4], acc[i][j][4 * g4 + 1]};
        f32x2 hi = {acc[i][j][4 * g4 + 2], acc[i][j][4 * g4 + 3]};
        if (SWAP) {
          const int m = m0 + wm * (BM / 2) + i * 32 + r;
          const int n = n0 + wn * (BN / 2) + j * 32 + 8 * g4 + 4 * hh;
          epi.vecN(m, n, lo, hi);
        } else {
          const int m = m0 + wm * (BM / 2) + i * 32 + 8 * g4 + 4 * hh;
          const int n = n0 + wn * (BN / 2) + j * 32 + r;
          epi.vecM(m, n, lo, hi);
        }
      }
}

__device__ __forceinline__ u32x2 pack4(f32x2 lo, f32x2 hi) {
  u32x2 w = {__builtin_bit_cast(unsigned, __builtin_convertvector(lo, bf16x2)),
             __builtin_bit_cast(unsigned, __builtin_convertvector(hi, bf16x2))};
  return w;
}

struct EpiQKV {
  u16* qs;
  u16* kf;
  u16* vf;
  __device__ __forceinline__ void vecN(int m, int n, f32x2 lo, f32x2 hi) const {
    const int which = n / D_MODEL;
    const int c = n - which * D_MODEL;
    const int h = c / DHEAD, d = c - h * DHEAD;
    const int key = m & 31;
    if (which == 0) {
      const float sc = 0.22360679774997896f * 1.4426950408889634f;
      *(u32x2*)&qs[(size_t)m * D_MODEL + c] = pack4(lo * sc, hi * sc);
    } else {
      const size_t tbase = (size_t)(h * 64 + (m >> 5)) * 640;
      const int off = (d < 16) ? ((key + 32 * (d >> 3)) * 8 + (d & 7)) : (512 + key * 4);
      *(u32x2*)&kf[tbase + off] = pack4(lo, hi);
    }
  }
  __device__ __forceinline__ void vecM(int m, int n, f32x2 lo, f32x2 hi) const {
    const int c = n - 2 * D_MODEL;
    const int h = c / DHEAD, d = c - h * DHEAD;
    const int key = m & 31;
    const int s = key >> 4, w = key & 15;
    const int hh = (w >> 2) & 1, j0 = 4 * (w >> 3);
    const size_t tbase = (size_t)(h * 64 + (m >> 5)) * 640;
    *(u32x2*)&vf[tbase + ((s * 2 + hh) * 20 + d) * 8 + j0] = pack4(lo, hi);
  }
};

struct EpiOut {
  u16* out;
  __device__ __forceinline__ void vecN(int m, int n, f32x2 lo, f32x2 hi) const {
    *(u32x2*)&out[(size_t)m * D_MODEL + n] = pack4(lo, hi);
  }
  __device__ __forceinline__ void vecM(int m, int n, f32x2 lo, f32x2 hi) const {}
};

__global__ __launch_bounds__(256, 2) void qkv_kernel(const u16* __restrict__ A, const u16* __restrict__ B, EpiQKV epi) {
  constexpr int BM = 128, BN = 128;
  __shared__ __attribute__((aligned(16))) u16 As[2 * BM * 72];
  __shared__ __attribute__((aligned(16))) u16 Bs[2 * 64 * (BN + 32)];
  const int nTN = N_QKV / BN;
  const int xcd = blockIdx.x & 7, idx = blockIdx.x >> 3;
  const int m0 = (xcd * 2 + idx / nTN) * BM, n0 = (idx % nTN) * BN;
  if (n0 < 2 * D_MODEL) gemm_body<BM, BN, true>(A, B, N_QKV, D_MODEL, m0, n0, epi, As, Bs);
  else gemm_body<BM, BN, false>(A, B, N_QKV, D_MODEL, m0, n0, epi, As, Bs);
}

__global__ __launch_bounds__(256, 2) void oproj_kernel(const u16* __restrict__ A, const u16* __restrict__ B, EpiOut epi) {
  constexpr int BM = 64, BN = 64;
  __shared__ __attribute__((aligned(16))) u16 As[2 * BM * 72];
  __shared__ __attribute__((aligned(16))) u16 Bs[2 * 64 * (BN + 32)];
  const int nTN = D_MODEL / BN;
  const int xcd = blockIdx.x & 7, idx = blockIdx.x >> 3;
  const int m0 = (xcd * 4 + idx / nTN) * BM, n0 = (idx % nTN) * BN;
  gemm_body<BM, BN, true>(A, B, D_MODEL, D_MODEL, m0, n0, epi, As, Bs);
}

#define CH_KEYS 256
#define CH_TILES 8
#define NCHUNK (S_LEN / CH_KEYS)

__device__ __forceinline__ float max3f(float a, float b, float c) { return fmaxf(fmaxf(a, b), c); }
__device__ __forceinline__ float max16(const f32x16& v) {
  const float a = max3f(v[0], v[1], v[2]), b = max3f(v[3], v[4], v[5]), c = max3f(v[6], v[7], v[8]);
  const float d = max3f(v[9], v[10], v[11]), e = max3f(v[12], v[13], v[14]);
  return fmaxf(max3f(a, b, c), max3f(d, e, v[15]));
}

__device__ __forceinline__ bf16x8 ld_kf0(const u16* Kt, int lane) { return *(const bf16x8*)&Kt[lane * 8]; }
__device__ __forceinline__ bf16x8 ld_kf1(const u16* Kt, int r) {
  const u32x2 k1 = *(const u32x2*)&Kt[512 + r * 4];
  const u32x4 k1v = {k1[0], k1[1], 0u, 0u};
  return __builtin_bit_cast(bf16x8, k1v);
}

__global__ __launch_bounds__(256, 2) void attn_kernel(const u16* __restrict__ qs, const u16* __restrict__ kf,
                                                      const u16* __restrict__ vf, u16* __restrict__ o) {
  __shared__ __attribute__((aligned(16))) u16 Ks[2][CH_TILES * 640];
  __shared__ __attribute__((aligned(16))) u16 Vs[2][CH_TILES * 4 * 32 * 8];

  const int tid = threadIdx.x, lane = tid & 63, wave = tid >> 6;
  const int r = lane & 31, hh = lane >> 5;
  const int xcd = blockIdx.x & 7, idx = blockIdx.x >> 3;
  const int h = xcd * 4 + (idx >> 4), qb = idx & 15;
  const int q = qb * 128 + wave * 32 + r;

  const u16* kg = kf + (size_t)h * 64 * 640;
  const u16* vg = vf + (size_t)h * 64 * 640;
  u32x4 rk[3], rv[3];
  auto gload = [&](int c) {
#pragma unroll
    for (int i = 0; i < 3; ++i) {
      const int id = tid + 256 * i;
      if (id < 640) {
        rk[i] = *(const u32x4*)&kg[(size_t)c * 5120 + id * 8];
        rv[i] = *(const u32x4*)&vg[(size_t)c * 5120 + id * 8];
      }
    }
  };
  auto lstore = [&](int buf) {
#pragma unroll
    for (int i = 0; i < 3; ++i) {
      const int id = tid + 256 * i;
      if (id < 640) {
        *(u32x4*)&Ks[buf][id * 8] = rk[i];
        const int ts = id / 20, d = id % 20;
        *(u32x4*)&Vs[buf][(ts * 32 + d) * 8] = rv[i];
      }
    }
  };
  for (int i = tid; i < 2 * CH_TILES * 4 * 12; i += 256) {
    const int buf = i / (CH_TILES * 4 * 12), rem = i % (CH_TILES * 4 * 12);
    const int ts = rem / 12, d = 20 + rem % 12;
    const unsigned val = (d == 20) ? 0x3F803F80u : 0u;
    u32x4 v4 = {val, val, val, val};
    *(u32x4*)&Vs[buf][(ts * 32 + d) * 8] = v4;
  }

  bf16x8 qf0, qf1;
  {
    const u16* qp = qs + (size_t)q * D_MODEL + h * DHEAD;
    u32x2 a0 = *(const u32x2*)(qp + 8 * hh);
    u32x2 a1 = *(const u32x2*)(qp + 8 * hh + 4);
    u32x4 t0 = {a0[0], a0[1], a1[0], a1[1]};
    qf0 = __builtin_bit_cast(bf16x8, t0);
    u32x2 b0 = *(const u32x2*)(qp + 16);
    u32x4 t1 = {hh ? 0u : b0[0], hh ? 0u : b0[1], 0u, 0u};
    qf1 = __builtin_bit_cast(bf16x8, t1);
  }

  f32x16 oacc, cvec, scur, snext;
  auto run = [&](auto safe_tag) {
  constexpr bool SAFE = decltype(safe_tag)::value;
  gload(0);
#pragma unroll
  for (int e = 0; e < 16; ++e) { oacc[e] = 0.f; cvec[e] = 0.f; }
  const float THR = 6.0f;

  lstore(0);
  __syncthreads();

  float mrun;
  {
    scur = MFMA32(ld_kf0(&Ks[0][0], lane), qf0, cvec);
    scur = MFMA32(ld_kf1(&Ks[0][0], r), qf1, scur);
    float tmax = max16(scur);
    mrun = fmaxf(tmax, __shfl_xor(tmax, 32));
#pragma unroll
    for (int e = 0; e < 16; ++e) { scur[e] -= mrun; cvec[e] = -mrun; }
  }

  for (int c = 0; c < NCHUNK; ++c) {
    const int buf = c & 1;
    if (c + 1 < NCHUNK) gload(c + 1);
    bf16x8 kA = ld_kf0(&Ks[buf][640], lane), kB = ld_kf1(&Ks[buf][640], r);
    bf16x8 vA = *(const bf16x8*)&Vs[buf][((0 + hh) * 32 + r) * 8];
    bf16x8 vB = *(const bf16x8*)&Vs[buf][((2 + hh) * 32 + r) * 8];
#pragma unroll
    for (int t = 0; t < CH_TILES; ++t) {
      if (t + 1 < CH_TILES) {
        snext = MFMA32(kA, qf0, cvec);
        snext = MFMA32(kB, qf1, snext);
      }
      const bf16x8 vf0 = vA, vf1 = vB;
      if (t + 2 < CH_TILES) {
        kA = ld_kf0(&Ks[buf][(t + 2) * 640], lane);
        kB = ld_kf1(&Ks[buf][(t + 2) * 640], r);
      }
      if (t + 1 < CH_TILES) {
        vA = *(const bf16x8*)&Vs[buf][(((t + 1) * 4 + 0 + hh) * 32 + r) * 8];
        vB = *(const bf16x8*)&Vs[buf][(((t + 1) * 4 + 2 + hh) * 32 + r) * 8];
      }
      float tmax = SAFE ? max16(scur) : 0.f;
      if (SAFE && __any(tmax > THR)) {
        float dm = fmaxf(fmaxf(tmax, __shfl_xor(tmax, 32)), 0.f);
        const float alpha = __builtin_amdgcn_exp2f(-dm);
        mrun += dm;
#pragma unroll
        for (int e = 0; e < 16; ++e) {
          oacc[e] *= alpha;
          scur[e] -= dm;
          if (t + 1 < CH_TILES) snext[e] -= dm;
          cvec[e] = -mrun;
        }
      }
      typedef __attribute__((ext_vector_type(4))) unsigned u4;
      u4 p0, p1;
#pragma unroll
      for (int e = 0; e < 4; ++e) {
        f32x2 a = {__builtin_amdgcn_exp2f(scur[2 * e]), __builtin_amdgcn_exp2f(scur[2 * e + 1])};
        p0[e] = __builtin_bit_cast(unsigned, __builtin_convertvector(a, bf16x2));
        f32x2 b = {__builtin_amdgcn_exp2f(scur[8 + 2 * e]), __builtin_amdgcn_exp2f(scur[8 + 2 * e + 1])};
        p1[e] = __builtin_bit_cast(unsigned, __builtin_convertvector(b, bf16x2));
      }
      oacc = MFMA32(vf0, __builtin_bit_cast(bf16x8, p0), oacc);
      oacc = MFMA32(vf1, __builtin_bit_cast(bf16x8, p1), oacc);
      if (t + 1 < CH_TILES) scur = snext;
    }
    if (c + 1 < NCHUNK) {
      lstore(buf ^ 1);
      __syncthreads();
      scur = MFMA32(ld_kf0(&Ks[buf ^ 1][0], lane), qf0, cvec);
      scur = MFMA32(ld_kf1(&Ks[buf ^ 1][0], r), qf1, scur);
    }
  }

  };
  run(std::false_type{});
  {
    bool bad = false;
#pragma unroll
    for (int e = 0; e < 16; ++e) bad |= !(fabsf(oacc[e]) < 3.0e38f);
    if (__syncthreads_or(bad ? 1 : 0)) run(std::true_type{});
  }

  float l = __shfl(oacc[8], r + 32);
  const float inv = 1.0f / l;
  u16* op = o + (size_t)q * D_MODEL + h * DHEAD;
#pragma unroll
  for (int gq = 0; gq < 3; ++gq) {
    if (gq < 2 || hh == 0) {
      f32x2 a = {oacc[4 * gq] * inv, oacc[4 * gq + 1] * inv};
      f32x2 b = {oacc[4 * gq + 2] * inv, oacc[4 * gq + 3] * inv};
      u32x2 w = {__builtin_bit_cast(unsigned, __builtin_convertvector(a, bf16x2)),
                 __builtin_bit_cast(unsigned, __builtin_convertvector(b, bf16x2))};
      *(u32x2*)(op + 8 * gq + 4 * hh) = w;
    }
  }
}

extern "C" void kernel_launch(void* const* d_in, const int* in_sizes, int n_in, void* d_out, int out_size,
                              void* d_ws, size_t ws_size, hipStream_t stream) {
  const u16* x = (const u16*)d_in[0];
  const u16* wqkv = (const u16*)d_in[1];
  const u16* wo = (const u16*)d_in[2];
  u16* out = (u16*)d_out;
  u16* ws = (u16*)d_ws;
  u16* qs = ws;
  u16* kf = qs + (size_t)S_LEN * D_MODEL;
  u16* vf = kf + (size_t)S_LEN * D_MODEL;
  u16* ob = vf + (size_t)S_LEN * D_MODEL;

  EpiQKV e1{qs, kf, vf};
  qkv_kernel<<<dim3((S_LEN / 128) * (N_QKV / 128)), dim3(256), 0, stream>>>(x, wqkv, e1);
  attn_kernel<<<dim3(NHEAD * (S_LEN / 128)), dim3(256), 0, stream>>>(qs, kf, vf, ob);
  EpiOut e3{out};
  oproj_kernel<<<dim3((S_LEN / 64) * (D_MODEL / 64)), dim3(256), 0, stream>>>(ob, wo, e3);
}
```

```cpp
#include <hip/hip_runtime.h>
#include <hip/hip_bf16.h>
#include <stdint.h>
#include <type_traits>

typedef unsigned short u16;
typedef __attribute__((ext_vector_type(8))) __bf16 bf16x8;
typedef __attribute__((ext_vector_type(4))) __bf16 bf16x4;
typedef __attribute__((ext_vector_type(2))) __bf16 bf16x2;
typedef __attribute__((ext_vector_type(4))) short s16x4;
typedef __attribute__((ext_vector_type(16))) float f32x16;
typedef __attribute__((ext_vector_type(2))) float f32x2;
typedef __attribute__((ext_vector_type(4))) unsigned u32x4;
typedef __attribute__((ext_vector_type(2))) unsigned u32x2;

#define S_LEN 2048
#define D_MODEL 640
#define NHEAD 32
#define DHEAD 20
#define N_QKV 1920

#define MFMA32(a, b, c) __builtin_amdgcn_mfma_f32_32x32x16_bf16((a), (b), (c), 0, 0, 0)

__device__ __forceinline__ u16 f2bf(float f) {
  __bf16 b = (__bf16)f;
  return __builtin_bit_cast(u16, b);
}

__device__ __forceinline__ u32x2 pack4(f32x2 lo, f32x2 hi);
template <int BM, int BN, bool SWAP, class Epi, bool LDSEPI = false>
__device__ __forceinline__ void gemm_body(const u16* __restrict__ A, const u16* __restrict__ B, int N, int K,
                                          int m0, int n0, const Epi& epi, u16* As, u16* Bs) {
  constexpr int BK = 64;
  constexpr int AS = BK + 8;
  constexpr int BS = BN + 32;
  constexpr int TM = BM / 64;
  constexpr int TN = BN / 64;
  constexpr int ACH = BM * BK / 8 / 256;
  constexpr int BCH = BK * BN / 8 / 256;

  const int tid = threadIdx.x;
  const int lane = tid & 63;
  const int wave = tid >> 6;
  const int wm = wave >> 1, wn = wave & 1;
  const int r = lane & 31, hh = lane >> 5;

  u32x4 ra[2][ACH], rb[2][BCH];
  auto gload = [&](int k0, int sl) {
#pragma unroll
    for (int i = 0; i < ACH; ++i) {
      int c = tid + 256 * i;
      int row = c >> 3, c8 = c & 7;
      ra[sl][i] = *(const u32x4*)&A[(size_t)(m0 + row) * K + k0 + c8 * 8];
    }
#pragma unroll
    for (int i = 0; i < BCH; ++i) {
      int c = tid + 256 * i;
      int row = c / (BN / 8), c8 = c % (BN / 8);
      rb[sl][i] = *(const u32x4*)&B[(size_t)(k0 + row) * N + n0 + c8 * 8];
    }
  };
  auto lstore = [&](int buf, int sl) {
#pragma unroll
    for (int i = 0; i < ACH; ++i) {
      int c = tid + 256 * i;
      int row = c >> 3, c8 = c & 7;
      *(u32x4*)&As[buf * (BM * AS) + row * AS + c8 * 8] = ra[sl][i];
    }
#pragma unroll
    for (int i = 0; i < BCH; ++i) {
      int c = tid + 256 * i;
      int row = c / (BN / 8), c8 = c % (BN / 8);
      *(u32x4*)&Bs[buf * (BK * BS) + row * BS + c8 * 8] = rb[sl][i];
    }
  };

  f32x16 acc[TM][TN];
#pragma unroll
  for (int i = 0; i < TM; ++i)
#pragma unroll
    for (int j = 0; j < TN; ++j)
#pragma unroll
      for (int e = 0; e < 16; ++e) acc[i][j][e] = 0.f;

  const int g = lane >> 4, gi = lane & 15;
  const int tq = gi >> 2, tp = gi & 3;

  const int nk = K / BK;
  gload(0, 0);
  gload(BK, 1);
  lstore(0, 0);
  __syncthreads();
  for (int kt = 0; kt < nk; ++kt) {
    const int buf = kt & 1;
    if (kt + 2 < nk) gload((kt + 2) * BK, buf);
#pragma unroll
    for (int ks = 0; ks < BK / 16; ++ks) {
      bf16x8 a[TM], b[TN];
#pragma unroll
      for (int i = 0; i < TM; ++i)
        a[i] = *(const bf16x8*)&As[buf * (BM * AS) + (wm * (BM / 2) + i * 32 + r) * AS + ks * 16 + 8 * hh];
#pragma unroll
      for (int j = 0; j < TN; ++j) {
        const int col = wn * (BN / 2) + j * 32 + (g & 1) * 16 + 4 * tp;
        const int krow = ks * 16 + 8 * (g >> 1) + tq;
        s16x4 lo = __builtin_amdgcn_ds_read_tr16_b64_v4i16(
            (__attribute__((address_space(3))) s16x4*)&Bs[buf * (BK * BS) + krow * BS + col]);
        s16x4 hi = __builtin_amdgcn_ds_read_tr16_b64_v4i16(
            (__attribute__((address_space(3))) s16x4*)&Bs[buf * (BK * BS) + (krow + 4) * BS + col]);
        typedef __attribute__((ext_vector_type(8))) short s16x8;
        s16x8 t = {lo[0], lo[1], lo[2], lo[3], hi[0], hi[1], hi[2], hi[3]};
        b[j] = __builtin_bit_cast(bf16x8, t);
      }
#pragma unroll
      for (int i = 0; i < TM; ++i)
#pragma unroll
        for (int j = 0; j < TN; ++j)
          acc[i][j] = SWAP ? MFMA32(b[j], a[i], acc[i][j]) : MFMA32(a[i], b[j], acc[i][j]);
    }
    if (kt + 1 < nk) lstore(buf ^ 1, buf ^ 1);
    __syncthreads();
  }

  if (LDSEPI) {
    constexpr int CS = BN + 8;
    u16* Cs = As;
#pragma unroll
    for (int i = 0; i < TM; ++i)
#pragma unroll
      for (int j = 0; j < TN; ++j)
#pragma unroll
        for (int g4 = 0; g4 < 4; ++g4) {
          f32x2 lo = {acc[i][j][4 * g4], acc[i][j][4 * g4 + 1]};
          f32x2 hi = {acc[i][j][4 * g4 + 2], acc[i][j][4 * g4 + 3]};
          *(u32x2*)&Cs[(wm * (BM / 2) + i * 32 + r) * CS + wn * (BN / 2) + j * 32 + 8 * g4 + 4 * hh] = pack4(lo, hi);
        }
    __syncthreads();
#pragma unroll
    for (int i = 0; i < BM * BN / 8 / 256; ++i) {
      const int c = tid + 256 * i;
      const int row = c / (BN / 8), c8 = c % (BN / 8);
      epi.row16(m0 + row, n0 + c8 * 8, *(const u32x4*)&Cs[row * CS + c8 * 8]);
    }
    return;
  }
#pragma unroll
  for (int i = 0; i < TM; ++i)
#pragma unroll
    for (int j = 0; j < TN; ++j)
#pragma unroll
      for (int g4 = 0; g4 < 4; ++g4) {
        f32x2 lo = {acc[i][j][4 * g4], acc[i][j][4 * g4 + 1]};
        f32x2 hi = {acc[i][j][4 * g4 + 2], acc[i][j][4 * g4 + 3]};
        if (SWAP) {
          const int m = m0 + wm * (BM / 2) + i * 32 + r;
          const int n = n0 + wn * (BN / 2) + j * 32 + 8 * g4 + 4 * hh;
          epi.vecN(m, n, lo, hi);
        } else {
          const int m = m0 + wm * (BM / 2) + i * 32 + 8 * g4 + 4 * hh;
          const int n = n0 + wn * (BN / 2) + j * 32 + r;
          epi.vecM(m, n, lo, hi);
        }
      }
}

__device__ __forceinline__ u32x2 pack4(f32x2 lo, f32x2 hi) {
  u32x2 w = {__builtin_bit_cast(unsigned, __builtin_convertvector(lo, bf16x2)),
             __builtin_bit_cast(unsigned, __builtin_convertvector(hi, bf16x2))};
  return w;
}

struct ArgQKV {
  u16* qs;
  u16* kf;
  u16* vf;
};
struct EpiQKV {
  float sc;
  u16* qs;
  u16* kf;
  u16* vf;
  __device__ __forceinline__ void row16(int m, int n, u32x4 v) const {}
  __device__ __forceinline__ void vecN(int m, int n, f32x2 lo, f32x2 hi) const {
    const int which = n / D_MODEL;
    const int c = n - which * D_MODEL;
    const int h = c / DHEAD, d = c - h * DHEAD;
    const int key = m & 31;
    const size_t tbase = (size_t)(h * 64 + (m >> 5)) * 640;
    const int off = (d < 16) ? ((key + 32 * (d >> 3)) * 8 + (d & 7)) : (512 + key * 4);
    *(u32x2*)&qs[tbase + off] = pack4(lo * sc, hi * sc);
  }
  __device__ __forceinline__ void vecM(int m, int n, f32x2 lo, f32x2 hi) const {
    const int c = n - 2 * D_MODEL;
    const int h = c / DHEAD, d = c - h * DHEAD;
    const int key = m & 31;
    const int s = key >> 4, w = key & 15;
    const int hh = (w >> 2) & 1, j0 = 4 * (w >> 3);
    const size_t tbase = (size_t)(h * 64 + (m >> 5)) * 640;
    *(u32x2*)&vf[tbase + ((s * 2 + hh) * 20 + d) * 8 + j0] = pack4(lo, hi);
  }
};

struct EpiOut {
  u16* out;
  __device__ __forceinline__ void row16(int m, int n, u32x4 v) const { *(u32x4*)&out[(size_t)m * D_MODEL + n] = v; }
  __device__ __forceinline__ void vecN(int m, int n, f32x2 lo, f32x2 hi) const {
    *(u32x2*)&out[(size_t)m * D_MODEL + n] = pack4(lo, hi);
  }
  __device__ __forceinline__ void vecM(int m, int n, f32x2 lo, f32x2 hi) const {}
};

__global__ __launch_bounds__(256, 2) void qkv_kernel(const u16* __restrict__ A, const u16* __restrict__ B, ArgQKV arg) {
  constexpr int BM = 128, BN = 128;
  __shared__ __attribute__((aligned(16))) u16 As[2 * BM * 72];
  __shared__ __attribute__((aligned(16))) u16 Bs[2 * 64 * (BN + 32)];
  const int nTN = N_QKV / BN;
  const int xcd = blockIdx.x & 7, idx = blockIdx.x >> 3;
  const int m0 = (xcd * 2 + idx / nTN) * BM, n0 = (idx % nTN) * BN;
  EpiQKV epi;
  epi.sc = (n0 < D_MODEL) ? 0.22360679774997896f * 1.4426950408889634f : 1.0f;
  epi.qs = (n0 < D_MODEL) ? arg.qs : arg.kf;
  epi.kf = arg.kf;
  epi.vf = arg.vf;
  if (n0 < 2 * D_MODEL) gemm_body<BM, BN, true>(A, B, N_QKV, D_MODEL, m0, n0, epi, As, Bs);
  else gemm_body<BM, BN, false>(A, B, N_QKV, D_MODEL, m0, n0, epi, As, Bs);
}

__global__ __launch_bounds__(256, 2) void oproj_kernel(const u16* __restrict__ A, const u16* __restrict__ B, EpiOut epi) {
  constexpr int BM = 64, BN = 64;
  __shared__ __attribute__((aligned(16))) u16 As[2 * BM * 72];
  __shared__ __attribute__((aligned(16))) u16 Bs[2 * 64 * (BN + 32)];
  const int nTN = D_MODEL / BN;
  const int xcd = blockIdx.x & 7, idx = blockIdx.x >> 3;
  const int m0 = (xcd * 4 + idx / nTN) * BM, n0 = (idx % nTN) * BN;
  gemm_body<BM, BN, true, EpiOut, true>(A, B, D_MODEL, D_MODEL, m0, n0, epi, As, Bs);
}

#define CH_KEYS 256
#define CH_TILES 8
#define NCHUNK (S_LEN / CH_KEYS)

__device__ __forceinline__ float max3f(float a, float b, float c) { return fmaxf(fmaxf(a, b), c); }
__device__ __forceinline__ float max16(const f32x16& v) {
  const float a = max3f(v[0], v[1], v[2]), b = max3f(v[3], v[4], v[5]), c = max3f(v[6], v[7], v[8]);
  const float d = max3f(v[9], v[10], v[11]), e = max3f(v[12], v[13], v[14]);
  return fmaxf(max3f(a, b, c), max3f(d, e, v[15]));
}

__device__ __forceinline__ bf16x8 ld_kf0(const u16* Kt, int lane) { return *(const bf16x8*)&Kt[lane * 8]; }
__device__ __forceinline__ bf16x8 ld_kf1(const u16* Kt, int r) {
  const u32x2 k1 = *(const u32x2*)&Kt[512 + r * 4];
  const u32x4 k1v = {k1[0], k1[1], 0u, 0u};
  return __builtin_bit_cast(bf16x8, k1v);
}

__global__ __launch_bounds__(256, 2) void attn_kernel(const u16* __restrict__ qs, const u16* __restrict__ kf,
                                                      const u16* __restrict__ vf, u16* __restrict__ o) {
  __shared__ __attribute__((aligned(16))) u16 Ks[2][CH_TILES * 640];
  __shared__ __attribute__((aligned(16))) u16 Vs[2][CH_TILES * 4 * 32 * 8];

  const int tid = threadIdx.x, lane = tid & 63, wave = tid >> 6;
  const int r = lane & 31, hh = lane >> 5;
  const int xcd = blockIdx.x & 7, idx = blockIdx.x >> 3;
  const int h = xcd * 4 + (idx >> 4), qb = idx & 15;
  const int q = qb * 128 + wave * 32 + r;

  const u16* kg = kf + (size_t)h * 64 * 640;
  const u16* vg = vf + (size_t)h * 64 * 640;
  u32x4 rk[3], rv[3];
  auto gload = [&](int c) {
#pragma unroll
    for (int i = 0; i < 3; ++i) {
      const int id = tid + 256 * i;
      if (id < 640) {
        rk[i] = *(const u32x4*)&kg[(size_t)c * 5120 + id * 8];
        rv[i] = *(const u32x4*)&vg[(size_t)c * 5120 + id * 8];
      }
    }
  };
  auto lstore = [&](int buf) {
#pragma unroll
    for (int i = 0; i < 3; ++i) {
      const int id = tid + 256 * i;
      if (id < 640) {
        *(u32x4*)&Ks[buf][id * 8] = rk[i];
        const int ts = id / 20, d = id % 20;
        *(u32x4*)&Vs[buf][(ts * 32 + d) * 8] = rv[i];
      }
    }
  };
  for (int i = tid; i < 2 * CH_TILES * 4 * 12; i += 256) {
    const int buf = i / (CH_TILES * 4 * 12), rem = i % (CH_TILES * 4 * 12);
    const int ts = rem / 12, d = 20 + rem % 12;
    const unsigned val = (d == 20) ? 0x3F803F80u : 0u;
    u32x4 v4 = {val, val, val, val};
    *(u32x4*)&Vs[buf][(ts * 32 + d) * 8] = v4;
  }

  bf16x8 qf0, qf1;
  {
    const u16* qp = qs + (size_t)(h * 64 + qb * 4 + wave) * 640;
    qf0 = *(const bf16x8*)(qp + lane * 8);
    u32x2 b0 = *(const u32x2*)(qp + 512 + r * 4);
    u32x4 t1 = {hh ? 0u : b0[0], hh ? 0u : b0[1], 0u, 0u};
    qf1 = __builtin_bit_cast(bf16x8, t1);
  }

  f32x16 oacc, cvec, scur, snext;
  auto run = [&](auto safe_tag) {
  constexpr bool SAFE = decltype(safe_tag)::value;
  gload(0);
#pragma unroll
  for (int e = 0; e < 16; ++e) { oacc[e] = 0.f; cvec[e] = 0.f; }
  const float THR = 6.0f;

  lstore(0);
  __syncthreads();

  float mrun;
  {
    scur = MFMA32(ld_kf0(&Ks[0][0], lane), qf0, cvec);
    scur = MFMA32(ld_kf1(&Ks[0][0], r), qf1, scur);
    float tmax = max16(scur);
    mrun = fmaxf(tmax, __shfl_xor(tmax, 32));
#pragma unroll
    for (int e = 0; e < 16; ++e) { scur[e] -= mrun; cvec[e] = -mrun; }
  }

  for (int c = 0; c < NCHUNK; ++c) {
    const int buf = c & 1;
    if (c + 1 < NCHUNK) gload(c + 1);
    bf16x8 kA = ld_kf0(&Ks[buf][640], lane), kB = ld_kf1(&Ks[buf][640], r);
    bf16x8 vA = *(const bf16x8*)&Vs[buf][((0 + hh) * 32 + r) * 8];
    bf16x8 vB = *(const bf16x8*)&Vs[buf][((2 + hh) * 32 + r) * 8];
#pragma unroll
    for (int t = 0; t < CH_TILES; ++t) {
      if (t + 1 < CH_TILES) {
        snext = MFMA32(kA, qf0, cvec);
        snext = MFMA32(kB, qf1, snext);
      }
      const bf16x8 vf0 = vA, vf1 = vB;
      if (t + 2 < CH_TILES) {
        kA = ld_kf0(&Ks[buf][(t + 2) * 640], lane);
        kB = ld_kf1(&Ks[buf][(t + 2) * 640], r);
      }
      if (t + 1 < CH_TILES) {
        vA = *(const bf16x8*)&Vs[buf][(((t + 1) * 4 + 0 + hh) * 32 + r) * 8];
        vB = *(const bf16x8*)&Vs[buf][(((t + 1) * 4 + 2 + hh) * 32 + r) * 8];
      }
      float tmax = SAFE ? max16(scur) : 0.f;
      if (SAFE && __any(tmax > THR)) {
        float dm = fmaxf(fmaxf(tmax, __shfl_xor(tmax, 32)), 0.f);
        const float alpha = __builtin_amdgcn_exp2f(-dm);
        mrun += dm;
#pragma unroll
        for (int e = 0; e < 16; ++e) {
          oacc[e] *= alpha;
          scur[e] -= dm;
          if (t + 1 < CH_TILES) snext[e] -= dm;
          cvec[e] = -mrun;
        }
      }
      typedef __attribute__((ext_vector_type(4))) unsigned u4;
      u4 p0, p1;
#pragma unroll
      for (int e = 0; e < 4; ++e) {
        f32x2 a = {__builtin_amdgcn_exp2f(scur[2 * e]), __builtin_amdgcn_exp2f(scur[2 * e + 1])};
        p0[e] = __builtin_bit_cast(unsigned, __builtin_convertvector(a, bf16x2));
        f32x2 b = {__builtin_amdgcn_exp2f(scur[8 + 2 * e]), __builtin_amdgcn_exp2f(scur[8 + 2 * e + 1])};
        p1[e] = __builtin_bit_cast(unsigned, __builtin_convertvector(b, bf16x2));
      }
      oacc = MFMA32(vf0, __builtin_bit_cast(bf16x8, p0), oacc);
      oacc = MFMA32(vf1, __builtin_bit_cast(bf16x8, p1), oacc);
      if (t + 1 < CH_TILES) scur = snext;
    }
    if (c + 1 < NCHUNK) {
      lstore(buf ^ 1);
      __syncthreads();
      scur = MFMA32(ld_kf0(&Ks[buf ^ 1][0], lane), qf0, cvec);
      scur = MFMA32(ld_kf1(&Ks[buf ^ 1][0], r), qf1, scur);
    }
  }

  };
  run(std::false_type{});
  {
    bool bad = false;
#pragma unroll
    for (int e = 0; e < 16; ++e) bad |= !(fabsf(oacc[e]) < 3.0e38f);
    if (__syncthreads_or(bad ? 1 : 0)) run(std::true_type{});
  }

  float l = __shfl(oacc[8], r + 32);
  const float inv = 1.0f / l;
  u16* op = o + (size_t)q * D_MODEL + h * DHEAD;
#pragma unroll
  for (int gq = 0; gq < 3; ++gq) {
    if (gq < 2 || hh == 0) {
      f32x2 a = {oacc[4 * gq] * inv, oacc[4 * gq + 1] * inv};
      f32x2 b = {oacc[4 * gq + 2] * inv, oacc[4 * gq + 3] * inv};
      u32x2 w = {__builtin_bit_cast(unsigned, __builtin_convertvector(a, bf16x2)),
                 __builtin_bit_cast(unsigned, __builtin_convertvector(b, bf16x2))};
      *(u32x2*)(op + 8 * gq + 4 * hh) = w;
    }
  }
}

extern "C" void kernel_launch(void* const* d_in, const int* in_sizes, int n_in, void* d_out, int out_size,
                              void* d_ws, size_t ws_size, hipStream_t stream) {
  const u16* x = (const u16*)d_in[0];
  const u16* wqkv = (const u16*)d_in[1];
  const u16* wo = (const u16*)d_in[2];
  u16* out = (u16*)d_out;
  u16* ws = (u16*)d_ws;
  u16* qs = ws;
  u16* kf = qs + (size_t)S_LEN * D_MODEL;
  u16* vf = kf + (size_t)S_LEN * D_MODEL;
  u16* ob = vf + (size_t)S_LEN * D_MODEL;

  ArgQKV e1{qs, kf, vf};
  qkv_kernel<<<dim3((S_LEN / 128) * (N_QKV / 128)), dim3(256), 0, stream>>>(x, wqkv, e1);
  attn_kernel<<<dim3(NHEAD * (S_LEN / 128)), dim3(256), 0, stream>>>(qs, kf, vf, ob);
  EpiOut e3{out};
  oproj_kernel<<<dim3((S_LEN / 64) * (D_MODEL / 64)), dim3(256), 0, stream>>>(ob, wo, e3);
}
```
